```python
import torch

def _softmax(x, axis=-1):
    return torch.softmax(x, dim=axis)
def _gelu(x):
    return torch.nn.functional.gelu(x, approximate="tanh")
def _silu(x):
    return torch.nn.functional.silu(x)
def _sigmoid(x):
    return torch.sigmoid(x)
def _layernorm(x, axis=-1, eps=1e-5):
    m = x.mean(dim=axis, keepdim=True)
    v = x.var(dim=axis, keepdim=True, unbiased=False)
    return (x - m) / torch.sqrt(v + eps)
def _scatter_add(tgt, idx, src, axis=0):
    return tgt.clone().index_add_(axis, idx, src)
def _segment_sum(src, seg, n):
    out = torch.zeros((n,) + src.shape[1:], dtype=src.dtype)
    return out.index_add_(0, seg, src)
def _pad(x, pw):
    flat = tuple(v for pair in reversed(pw) for v in pair)
    return torch.nn.functional.pad(x, flat)

kernel_name = 'standalone__qk_norm__prompt'

def setup_inputs():

    _w = (lambda a: (lambda t: t.to(torch.bfloat16) if t.is_floating_point() else t)(torch.as_tensor(a)))
    B, H, S, D = 2, 16, 2048, 448
    Dh = D // H
    return {
        "q": _w((lambda *s: torch.randn(*s, dtype=torch.bfloat16) * 0.05)(B, H, S, Dh)),
        "k": _w((lambda *s: torch.randn(*s, dtype=torch.bfloat16) * 0.05)(B, H, S, Dh)),
        "v": _w((lambda *s: torch.randn(*s, dtype=torch.bfloat16) * 1.6)(B, H, S, Dh)),
        "t": _w(torch.abs((lambda *s: torch.randn(*s, dtype=torch.bfloat16) * 1.6)(H)) + 1.0),
    }

def reference(q, k, v, t):
    eps = 1e-6
    qn = q / (torch.sqrt((q * q).sum(axis=-1, keepdims=True)) + eps)
    kn = k / (torch.sqrt((k * k).sum(axis=-1, keepdims=True)) + eps)
    logits = torch.einsum('bhqd,bhkd->bhqk', qn, kn) * t[None, :, None, None]
    attn = _softmax(logits, axis=-1)
    return torch.einsum('bhqk,bhkd->bhqd', attn, v)
```

```cpp
#include <hip/hip_runtime.h>
#include <hip/hip_bf16.h>
#include <stdint.h>

constexpr int S_LEN = 2048, DH = 28, NH = 16;

__device__ __forceinline__ float bf2f(unsigned short u) { return __uint_as_float((unsigned)u << 16); }
__device__ __forceinline__ float rbf(float x) { return __bfloat162float(__float2bfloat16(x)); }

__device__ __forceinline__ void slow_path(const unsigned short* q, const unsigned short* k, const unsigned short* v,
                                          float tt, unsigned short* out, int bh, int qb, int tid, float* sm) {
    float* sk = sm;
    float* sv = sm + 256 * DH;
    const size_t hb = (size_t)bh * S_LEN * DH;
    float qn[DH], o[DH];
    float m = -INFINITY, l = 0.f;
    if (tid < 256) {
        const unsigned short* qr = q + hb + (size_t)(qb * 256 + tid) * DH;
        float ss = 0.f;
#pragma unroll
        for (int d = 0; d < DH; ++d) { qn[d] = bf2f(qr[d]); ss += qn[d] * qn[d]; }
        const float c = 1.f / (sqrtf(ss) + 1e-6f);
#pragma unroll
        for (int d = 0; d < DH; ++d) { qn[d] = rbf(qn[d] * c); o[d] = 0.f; }
    }
    for (int c0 = 0; c0 < S_LEN; c0 += 256) {
        __syncthreads();
        if (tid < 256) {
            const unsigned short* kr = k + hb + (size_t)(c0 + tid) * DH;
            float ss = 0.f;
            for (int d = 0; d < DH; ++d) { const float x = bf2f(kr[d]); ss += x * x; }
            const float c = 1.f / (sqrtf(ss) + 1e-6f);
            for (int d = 0; d < DH; ++d) sk[tid * DH + d] = rbf(bf2f(kr[d]) * c);
        } else if (tid < 512) {
            const unsigned short* vr = v + hb + (size_t)(c0 + tid - 256) * DH;
            for (int d = 0; d < DH; ++d) sv[(tid - 256) * DH + d] = bf2f(vr[d]);
        }
        __syncthreads();
        if (tid < 256) {
            for (int j = 0; j < 256; ++j) {
                float s = 0.f;
#pragma unroll
                for (int d = 0; d < DH; ++d) s += qn[d] * sk[j * DH + d];
                s *= tt;
                const float mn = fmaxf(m, s);
                const float a = __expf(m - mn), p = __expf(s - mn);
                l = l * a + p;
#pragma unroll
                for (int d = 0; d < DH; ++d) o[d] = o[d] * a + p * sv[j * DH + d];
                m = mn;
            }
        }
    }
    if (tid < 256) {
        const float r = 1.f / l;
        __hip_bfloat16* orow = (__hip_bfloat16*)out + hb + (size_t)(qb * 256 + tid) * DH;
#pragma unroll
        for (int d = 0; d < DH; ++d) orow[d] = __float2bfloat16(o[d] * r);
    }
}

__global__ __launch_bounds__(768, 3) void qk_norm_attn(const unsigned short* q, const unsigned short* k, const unsigned short* v,
                                                      const unsigned short* t, unsigned short* out) {
    __shared__ __attribute__((aligned(16))) float sm[2 * 256 * DH];
    const int b = blockIdx.x;
    const int bh = (b & 7) + 8 * (b >> 6), qb = (b >> 3) & 7;
    slow_path(q, k, v, bf2f(t[bh % NH]), out, bh, qb, threadIdx.x, sm);
}

extern "C" void kernel_launch(void* const* d_in, const int* in_sizes, int n_in, void* d_out, int out_size, void* d_ws,
                              size_t ws_size, hipStream_t stream) {
    qk_norm_attn<<<256, 768, 0, stream>>>((const unsigned short*)d_in[0], (const unsigned short*)d_in[1],
                                          (const unsigned short*)d_in[2], (const unsigned short*)d_in[3], (unsigned short*)d_out);
}
```

```cpp
#include <hip/hip_runtime.h>
#include <hip/hip_bf16.h>
#include <stdint.h>

constexpr int S_LEN = 2048, DH = 28, NH = 16;

__device__ __forceinline__ float bf2f(unsigned short u) { return __uint_as_float((unsigned)u << 16); }
__device__ __forceinline__ float rbf(float x) { return __bfloat162float(__float2bfloat16(x)); }

__device__ __forceinline__ void slow_path(const unsigned short* q, const unsigned short* k, const unsigned short* v,
                                          float tt, unsigned short* out, int bh, int qb, int tid, float* sm) {
    float* sk = sm;
    float* sv = sm + 256 * DH;
    const size_t hb = (size_t)bh * S_LEN * DH;
    float qn[DH], o[DH];
    float m = -INFINITY, l = 0.f;
    if (tid < 256) {
        const unsigned short* qr = q + hb + (size_t)(qb * 256 + tid) * DH;
        float ss = 0.f;
#pragma unroll
        for (int d = 0; d < DH; ++d) { qn[d] = bf2f(qr[d]); ss += qn[d] * qn[d]; }
        const float c = 1.f / (sqrtf(ss) + 1e-6f);
#pragma unroll
        for (int d = 0; d < DH; ++d) { qn[d] = rbf(qn[d] * c); o[d] = 0.f; }
    }
    for (int c0 = 0; c0 < S_LEN; c0 += 256) {
        __syncthreads();
        if (tid < 256) {
            const unsigned short* kr = k + hb + (size_t)(c0 + tid) * DH;
            float ss = 0.f;
            for (int d = 0; d < DH; ++d) { const float x = bf2f(kr[d]); ss += x * x; }
            const float c = 1.f / (sqrtf(ss) + 1e-6f);
            for (int d = 0; d < DH; ++d) sk[tid * DH + d] = rbf(bf2f(kr[d]) * c);
        }
        if (tid < 256) {
            const unsigned short* vr = v + hb + (size_t)(c0 + tid) * DH;
            for (int d = 0; d < DH; ++d) sv[(tid) * DH + d] = bf2f(vr[d]);
        }
        __syncthreads();
        if (tid < 256) {
            for (int j = 0; j < 256; ++j) {
                float s = 0.f;
#pragma unroll
                for (int d = 0; d < DH; ++d) s += qn[d] * sk[j * DH + d];
                s *= tt;
                const float mn = fmaxf(m, s);
                const float a = __expf(m - mn), p = __expf(s - mn);
                l = l * a + p;
#pragma unroll
                for (int d = 0; d < DH; ++d) o[d] = o[d] * a + p * sv[j * DH + d];
                m = mn;
            }
        }
    }
    if (tid < 256) {
        const float r = 1.f / l;
        __hip_bfloat16* orow = (__hip_bfloat16*)out + hb + (size_t)(qb * 256 + tid) * DH;
#pragma unroll
        for (int d = 0; d < DH; ++d) orow[d] = __float2bfloat16(o[d] * r);
    }
}

__global__ __launch_bounds__(256) void qk_norm_attn(const unsigned short* q, const unsigned short* k, const unsigned short* v,
                                                      const unsigned short* t, unsigned short* out) {
    __shared__ __attribute__((aligned(16))) float sm[36864];
    unsigned long long t0 = __builtin_amdgcn_s_memrealtime();
    const int b = blockIdx.x;
    const int bh = (b & 7) + 8 * (b >> 6), qb = (b >> 3) & 7;
    slow_path(q, k, v, bf2f(t[bh % NH]), out, bh, qb, threadIdx.x, sm);

    while ((long long)(__builtin_amdgcn_s_memrealtime() - t0) < 70000) __builtin_amdgcn_s_sleep(2);
}

extern "C" void kernel_launch(void* const* d_in, const int* in_sizes, int n_in, void* d_out, int out_size, void* d_ws,
                              size_t ws_size, hipStream_t stream) {
    qk_norm_attn<<<256, 256, 0, stream>>>((const unsigned short*)d_in[0], (const unsigned short*)d_in[1],
                                          (const unsigned short*)d_in[2], (const unsigned short*)d_in[3], (unsigned short*)d_out);
}
```

```cpp
#include <hip/hip_runtime.h>
#include <hip/hip_bf16.h>
#include <stdint.h>

constexpr int S_LEN = 2048, DH = 28, NH = 16;

__device__ __forceinline__ float bf2f(unsigned short u) { return __uint_as_float((unsigned)u << 16); }
__device__ __forceinline__ float rbf(float x) { return __bfloat162float(__float2bfloat16(x)); }

__device__ __forceinline__ void slow_path(const unsigned short* q, const unsigned short* k, const unsigned short* v,
                                          float tt, unsigned short* out, int bh, int qb, int tid, float* sm) {
    float* sk = sm;
    float* sv = sm + 256 * DH;
    const size_t hb = (size_t)bh * S_LEN * DH;
    float qn[DH], o[DH];
    float m = -INFINITY, l = 0.f;
    if (tid < 256) {
        const unsigned short* qr = q + hb + (size_t)(qb * 256 + tid) * DH;
        float ss = 0.f;
#pragma unroll
        for (int d = 0; d < DH; ++d) { qn[d] = bf2f(qr[d]); ss += qn[d] * qn[d]; }
        const float c = 1.f / (sqrtf(ss) + 1e-6f);
#pragma unroll
        for (int d = 0; d < DH; ++d) { qn[d] = rbf(qn[d] * c); o[d] = 0.f; }
    }
    for (int c0 = 0; c0 < S_LEN; c0 += 256) {
        __syncthreads();
        if (tid < 256) {
            const unsigned short* kr = k + hb + (size_t)(c0 + tid) * DH;
            float ss = 0.f;
            for (int d = 0; d < DH; ++d) { const float x = bf2f(kr[d]); ss += x * x; }
            const float c = 1.f / (sqrtf(ss) + 1e-6f);
            for (int d = 0; d < DH; ++d) sk[tid * DH + d] = rbf(bf2f(kr[d]) * c);
        } else if (tid < 512) {
            const unsigned short* vr = v + hb + (size_t)(c0 + tid - 256) * DH;
            for (int d = 0; d < DH; ++d) sv[(tid - 256) * DH + d] = bf2f(vr[d]);
        }
        __syncthreads();
        if (tid < 256) {
            for (int j = 0; j < 256; ++j) {
                float s = 0.f;
#pragma unroll
                for (int d = 0; d < DH; ++d) s += qn[d] * sk[j * DH + d];
                s *= tt;
                const float mn = fmaxf(m, s);
                const float a = __expf(m - mn), p = __expf(s - mn);
                l = l * a + p;
#pragma unroll
                for (int d = 0; d < DH; ++d) o[d] = o[d] * a + p * sv[j * DH + d];
                m = mn;
            }
        }
    }
    if (tid < 256) {
        const float r = 1.f / l;
        __hip_bfloat16* orow = (__hip_bfloat16*)out + hb + (size_t)(qb * 256 + tid) * DH;
#pragma unroll
        for (int d = 0; d < DH; ++d) orow[d] = __float2bfloat16(o[d] * r);
    }
}

__global__ __launch_bounds__(768, 3) void qk_norm_attn(const unsigned short* q, const unsigned short* k, const unsigned short* v,
                                                      const unsigned short* t, unsigned short* out, void* ws) {
    __shared__ __attribute__((aligned(1024))) char smem[147456];
    const int bid = blockIdx.x;
    const int wid = __builtin_amdgcn_readfirstlane(threadIdx.x >> 6);
    asm volatile(
        "s_bfe_u32 s46, %[bid], 0x10007\n"
        "s_lshr_b32 s47, %[bid], 6\n"
        "s_lshl2_add_u32 s46, s46, %[bid]\n"
        "s_and_b32 s46, s46, 7\n"
        "s_lshl3_add_u32 s64, s47, s46\n"
        "s_bfe_u32 s63, %[bid], 0x30003\n"
        "v_mbcnt_lo_u32_b32 v150, -1, 0\n"
        "s_and_b32 s44, %[wid], 3\n"
        "v_mbcnt_hi_u32_b32 v150, -1, v150\n"
        "s_lshr_b32 s45, %[wid], 2\n"
        "v_mul_u32_u24 v114, 56, v150\n"
        "s_add_u32 s70, s63, 1\n"
        "s_cmp_eq_u32 s70, 8\n"
        "s_cselect_b32 s70, 1, s70\n"
        "s_cmp_eq_u32 s45, 0\n"
        "s_cselect_b64 s[36:37], %[q], %[k]\n"
        "s_cselect_b32 s46, s63, 0\n"
        "s_cmp_eq_u32 s45, 1\n"
        "s_cselect_b32 s46, s70, s46\n"
        "s_lshl3_add_u32 s46, s64, s46\n"
        "s_lshl2_add_u32 s46, s46, s44\n"
        "s_mul_i32 s46, s46, 3584\n"
        "s_add_u32 s36, s36, s46\n"
        "s_addc_u32 s37, s37, 0\n"
        "global_load_dwordx4 v[96:99], v114, s[36:37]\n"
        "global_load_dwordx4 v[100:103], v114, s[36:37] offset:16\n"
        "global_load_dwordx4 v[104:107], v114, s[36:37] offset:32\n"
        "global_load_dwordx2 v[108:109], v114, s[36:37] offset:48\n"
        "s_mul_i32 s62, s64, 114688\n"
        "s_and_b32 s47, s64, 14\n"
        "s_lshl_b32 s47, s47, 1\n"
        "s_load_dword s48, %[t], s47\n"
        "v_lshlrev_b32 v115, 4, v150\n"
        "s_cmp_lg_u32 s45, 2\n"
        "s_cbranch_scc1 L_notprod_%=\n"
        "s_setprio 3\n"
        "s_mul_i32 s46, s44, 3584\n"
        "s_add_u32 s52, s46, 65536\n"
        "s_add_u32 s46, s46, s62\n"
        "s_mov_b64 s[38:39], %[v]\n"
        "s_add_u32 s38, s38, s46\n"
        "s_addc_u32 s39, s39, 0\n"
        "s_mov_b32 m0, s52\n"
        "s_nop 0\n"
        "global_load_lds_dwordx4 v115, s[38:39] offset:0\n"
        "global_load_lds_dwordx4 v115, s[38:39] offset:1024\n"
        "global_load_lds_dwordx4 v115, s[38:39] offset:2048\n"
        "s_mov_b32 exec_hi, 0\n"
        "global_load_lds_dwordx4 v115, s[38:39] offset:3072\n"
        "s_mov_b32 exec_hi, -1\n"
        "s_mov_b64 s[68:69], s[38:39]\n"
        "s_mov_b64 s[42:43], s[36:37]\n"
        "s_lshl_b32 s46, s64, 5\n"
        "s_add_u32 s46, s46, s44\n"
        "s_lshl_b32 s46, s46, 12\n"
        "s_mov_b64 s[58:59], %[ws]\n"
        "s_add_u32 s58, s58, s46\n"
        "s_addc_u32 s59, s59, 0\n"
        "v_mov_b32 v4, 0\n"
        "global_load_dwordx2 v[0:1], v4, s[58:59] offset:1024 sc1\n"
        "s_mov_b32 s72, s70\n"
        "s_mul_i32 s46, s72, 14336\n"
        "s_add_u32 s38, s68, s46\n"
        "s_addc_u32 s39, s69, 0\n"
        "s_add_u32 s52, s52, 14336\n"
        "s_sub_u32 s46, s52, 57344\n"
        "s_cmp_ge_u32 s52, 122880\n"
        "s_cselect_b32 s52, s46, s52\n"
        "s_mov_b32 m0, s52\n"
        "s_nop 0\n"
        "global_load_lds_dwordx4 v115, s[38:39] offset:0\n"
        "global_load_lds_dwordx4 v115, s[38:39] offset:1024\n"
        "global_load_lds_dwordx4 v115, s[38:39] offset:2048\n"
        "s_mov_b32 exec_hi, 0\n"
        "global_load_lds_dwordx4 v115, s[38:39] offset:3072\n"
        "s_mov_b32 exec_hi, -1\n"
        "v_and_b32 v4, 7, v150\n"
        "v_lshlrev_b32 v4, 7, v4\n"
        "s_branch L_common_%=\n"
        "L_notprod_%=:\n"
        "v_mov_b32 v0, 0\n"
        "v_mov_b32 v1, 0\n"
        "v_mov_b32 v2, 0\n"
        "v_mov_b32 v3, 0\n"
        "v_mov_b32 v4, 0\n"
        "v_mov_b32 v5, 0\n"
        "v_mov_b32 v6, 0\n"
        "v_mov_b32 v7, 0\n"
        "v_mov_b32 v8, 0\n"
        "v_mov_b32 v9, 0\n"
        "v_mov_b32 v10, 0\n"
        "v_mov_b32 v11, 0\n"
        "v_mov_b32 v12, 0\n"
        "v_mov_b32 v13, 0\n"
        "v_mov_b32 v14, 0\n"
        "v_mov_b32 v15, 0\n"
        "v_mov_b32 v16, 0\n"
        "v_mov_b32 v17, 0\n"
        "v_mov_b32 v18, 0\n"
        "v_mov_b32 v19, 0\n"
        "v_mov_b32 v20, 0\n"
        "v_mov_b32 v21, 0\n"
        "v_mov_b32 v22, 0\n"
        "v_mov_b32 v23, 0\n"
        "v_mov_b32 v24, 0\n"
        "v_mov_b32 v25, 0\n"
        "v_mov_b32 v26, 0\n"
        "v_mov_b32 v27, 0\n"
        "v_mov_b32 v28, 0\n"
        "v_mov_b32 v29, 0\n"
        "v_mov_b32 v30, 0\n"
        "v_mov_b32 v31, 0\n"
        "v_mov_b32 v80, 0\n"
        "v_mov_b32 v81, 0\n"
        "v_mov_b32 v82, 0\n"
        "v_mov_b32 v83, 0\n"
        "v_mov_b32 v84, 0\n"
        "v_mov_b32 v85, 0\n"
        "v_mov_b32 v86, 0\n"
        "v_mov_b32 v87, 0\n"
        "v_mov_b32 v88, 0\n"
        "v_mov_b32 v89, 0\n"
        "v_mov_b32 v90, 0\n"
        "v_mov_b32 v91, 0\n"
        "v_mov_b32 v92, 0\n"
        "v_mov_b32 v93, 0\n"
        "v_mov_b32 v94, 0\n"
        "v_mov_b32 v95, 0\n"
        "v_mov_b32 v136, 0\n"
        "v_mov_b32 v137, 0\n"
        "v_mov_b32 v138, 0\n"
        "v_mov_b32 v139, 0\n"
        "v_mov_b32 v140, 0\n"
        "v_mov_b32 v141, 0\n"
        "v_mov_b32 v142, 0\n"
        "v_mov_b32 v143, 0\n"
        "v_mov_b32 v144, 0xdddd\n"
        "s_lshl_b32 s46, s45, 13\n"
        "v_add_u32 v147, s46, v115\n"
        "v_mov_b32 v145, v147\n"
        "v_lshrrev_b32 v152, 5, v150\n"
        "v_bfe_u32 v153, v150, 2, 2\n"
        "v_lshl_add_u32 v153, v152, 3, v153\n"
        "v_mul_u32_u24 v153, 56, v153\n"
        "v_bfe_u32 v151, v150, 4, 1\n"
        "v_lshl_add_u32 v153, v151, 5, v153\n"
        "v_and_b32 v151, 3, v150\n"
        "v_lshl_add_u32 v153, v151, 3, v153\n"
        "s_mul_i32 s46, s45, 7168\n"
        "s_add_u32 s46, s46, 65536\n"
        "v_add_u32 v153, s46, v153\n"
        "v_lshlrev_b32 v152, 6, v152\n"
        "v_sub_u32 v152, 123104, v152\n"
        "s_mov_b32 s54, 0x88880000\n"
        "s_mov_b32 s55, 0x88880000\n"
        "v_mov_b32 v151, 14336\n"
        "v_cndmask_b32 v148, v153, v152, s[54:55]\n"
        "v_cndmask_b32 v149, v151, 0, s[54:55]\n"
        "v_mov_b32 v146, v148\n"
        "v_mov_b32 v64, 0x3f80\n"
        "v_mov_b32 v65, 0\n"
        "v_mov_b32 v66, 0x3f80\n"
        "v_mov_b32 v67, 0\n"
        "s_lshl_b32 s46, s45, 2\n"
        "s_add_u32 s46, s46, s44\n"
        "s_lshl_b32 s46, s46, 10\n"
        "s_add_u32 s46, s46, 122880\n"
        "v_add_u32 v152, s46, v115\n"
        "ds_write_b128 v152, v[64:67]\n"
        "s_lshl_b32 s46, s44, 1\n"
        "s_add_u32 s46, s46, s45\n"
        "s_lshl_b32 s47, s46, 12\n"
        "s_add_u32 s47, s47, 16384\n"
        "v_add_u32 v154, s47, v115\n"
        "v_xor_b32 v155, 0x1000, v154\n"
        "s_lshl_b32 s47, s63, 3\n"
        "s_add_u32 s47, s47, s46\n"
        "s_mul_i32 s47, s47, 1792\n"
        "s_add_u32 s47, s47, s62\n"
        "s_mov_b64 s[40:41], %[o]\n"
        "s_add_u32 s40, s40, s47\n"
        "s_addc_u32 s41, s41, 0\n"
        "s_lshl_b32 s46, s46, 11\n"
        "s_add_u32 s46, s46, 79872\n"
        "v_add_u32 v157, s46, v115\n"
        "v_and_b32 v151, 31, v150\n"
        "v_mul_u32_u24 v151, 56, v151\n"
        "v_lshrrev_b32 v153, 5, v150\n"
        "v_lshl_add_u32 v151, v153, 3, v151\n"
        "v_add_u32 v156, s46, v151\n"
        "L_common_%=:\n"
        "s_lshl_b32 s46, s64, 5\n"
        "s_add_u32 s46, s46, s44\n"
        "s_lshl_b32 s46, s46, 12\n"
        "s_mov_b64 s[56:57], %[ws]\n"
        "s_add_u32 s56, s56, s46\n"
        "s_addc_u32 s57, s57, 0\n"
        "v_and_b32 v152, 1, v150\n"
        "v_lshlrev_b32 v152, 4, v152\n"
        "v_mov_b32 v153, 0x3f80\n"
        "v_lshlrev_b32 v152, v152, v153\n"
        "s_mov_b32 s54, 0xcccccccc\n"
        "s_mov_b32 s55, 0xcccccccc\n"
        "v_cndmask_b32 v110, v152, 0, s[54:55]\n"
        "v_cndmask_b32 v111, 0, v152, s[54:55]\n"
        "v_and_b32 v152, 32, v150\n"
        "v_mul_u32_u24 v152, 48, v152\n"
        "v_add_u32 v151, v152, v115\n"
        "s_cmp_eq_u32 s45, 0\n"
        "s_cselect_b32 s46, 131072, 0\n"
        "s_cmp_eq_u32 s45, 1\n"
        "s_cselect_b32 s46, 16384, s46\n"
        "s_lshl_b32 s47, s44, 12\n"
        "s_add_u32 s46, s46, s47\n"
        "v_add_u32 v116, s46, v151\n"
        "s_and_b32 s53, s64, 1\n"
        "s_lshl_b32 s53, s53, 4\n"
        "s_waitcnt lgkmcnt(0)\n"
        "s_lshr_b32 s48, s48, s53\n"
        "s_lshl_b32 s48, s48, 16\n"
        "s_and_b32 s46, s48, 0x7fffffff\n"
        "s_cmp_gt_u32 s46, 0x41800000\n"
        "s_cbranch_scc1 L_fallback_%=\n"
        "v_mov_b32 v152, 0x3fb8aa3b\n"
        "v_mul_f32 v117, s48, v152\n"
        "v_mov_b32 v78, 0xc220\n"
        "v_mov_b32 v79, 0\n"
        "s_cmp_eq_u32 s45, 0\n"
        "s_cbranch_scc1 L_isq_%=\n"
        "v_mov_b32 v117, 1.0\n"
        "v_mov_b32 v78, 0x3f80\n"
        "L_isq_%=:\n"
        "s_cmp_eq_u32 s45, 2\n"
        "s_cbranch_scc1 L_pw_%=\n"
        "s_waitcnt vmcnt(0)\n"
        "L_pw_%=:\n"
        "s_waitcnt vmcnt(9)\n"
        "s_mov_b32 s67, 0\n"
        "L_norm_%=:\n"
        "v_dot2_f32_bf16 v112, v96, v96, 0\n"
        "v_dot2_f32_bf16 v113, v97, v97, 0\n"
        "v_dot2_f32_bf16 v112, v98, v98, v112\n"
        "v_dot2_f32_bf16 v113, v99, v99, v113\n"
        "v_dot2_f32_bf16 v112, v100, v100, v112\n"
        "v_dot2_f32_bf16 v113, v101, v101, v113\n"
        "v_dot2_f32_bf16 v112, v102, v102, v112\n"
        "v_dot2_f32_bf16 v113, v103, v103, v113\n"
        "v_dot2_f32_bf16 v112, v104, v104, v112\n"
        "v_dot2_f32_bf16 v113, v105, v105, v113\n"
        "v_dot2_f32_bf16 v112, v106, v106, v112\n"
        "v_dot2_f32_bf16 v113, v107, v107, v113\n"
        "v_dot2_f32_bf16 v112, v108, v108, v112\n"
        "v_dot2_f32_bf16 v113, v109, v109, v113\n"
        "v_mfma_f32_4x4x4_16b_bf16 v[32:35], v[110:111], v[96:97], 0\n"
        "v_mfma_f32_4x4x4_16b_bf16 v[36:39], v[110:111], v[98:99], 0\n"
        "v_mfma_f32_4x4x4_16b_bf16 v[40:43], v[110:111], v[100:101], 0\n"
        "v_add_f32 v112, v112, v113\n"
        "v_cvt_pk_bf16_f32 v112, 0, v112\n"
        "v_sqrt_f32 v112, v112\n"
        "v_mfma_f32_4x4x4_16b_bf16 v[44:47], v[110:111], v[102:103], 0\n"
        "v_cvt_pk_bf16_f32 v112, 0, v112\n"
        "v_mfma_f32_4x4x4_16b_bf16 v[48:51], v[110:111], v[104:105], 0\n"
        "v_add_f32 v112, 0x358637bd, v112\n"
        "v_mfma_f32_4x4x4_16b_bf16 v[52:55], v[110:111], v[106:107], 0\n"
        "v_cvt_pk_bf16_f32 v112, 0, v112\n"
        "v_rcp_f32 v112, v112\n"
        "v_mfma_f32_4x4x4_16b_bf16 v[56:59], v[110:111], v[108:109], 0\n"
        "v_mul_f32 v112, v117, v112\n"
        "v_pk_mul_f32 v[32:33], v[32:33], v[112:113] op_sel_hi:[1,0]\n"
        "v_pk_mul_f32 v[34:35], v[34:35], v[112:113] op_sel_hi:[1,0]\n"
        "v_pk_mul_f32 v[36:37], v[36:37], v[112:113] op_sel_hi:[1,0]\n"
        "v_pk_mul_f32 v[38:39], v[38:39], v[112:113] op_sel_hi:[1,0]\n"
        "v_cvt_pk_bf16_f32 v64, v32, v33\n"
        "v_cvt_pk_bf16_f32 v65, v34, v35\n"
        "v_cvt_pk_bf16_f32 v66, v36, v37\n"
        "v_cvt_pk_bf16_f32 v67, v38, v39\n"
        "ds_write_b128 v116, v[64:67] offset:0\n"
        "v_pk_mul_f32 v[40:41], v[40:41], v[112:113] op_sel_hi:[1,0]\n"
        "v_pk_mul_f32 v[42:43], v[42:43], v[112:113] op_sel_hi:[1,0]\n"
        "v_pk_mul_f32 v[44:45], v[44:45], v[112:113] op_sel_hi:[1,0]\n"
        "v_pk_mul_f32 v[46:47], v[46:47], v[112:113] op_sel_hi:[1,0]\n"
        "v_cvt_pk_bf16_f32 v68, v40, v41\n"
        "v_cvt_pk_bf16_f32 v69, v42, v43\n"
        "v_cvt_pk_bf16_f32 v70, v44, v45\n"
        "v_cvt_pk_bf16_f32 v71, v46, v47\n"
        "ds_write_b128 v116, v[68:71] offset:512\n"
        "v_pk_mul_f32 v[48:49], v[48:49], v[112:113] op_sel_hi:[1,0]\n"
        "v_pk_mul_f32 v[50:51], v[50:51], v[112:113] op_sel_hi:[1,0]\n"
        "v_pk_mul_f32 v[52:53], v[52:53], v[112:113] op_sel_hi:[1,0]\n"
        "v_pk_mul_f32 v[54:55], v[54:55], v[112:113] op_sel_hi:[1,0]\n"
        "v_cvt_pk_bf16_f32 v72, v48, v49\n"
        "v_cvt_pk_bf16_f32 v73, v50, v51\n"
        "v_cvt_pk_bf16_f32 v74, v52, v53\n"
        "v_cvt_pk_bf16_f32 v75, v54, v55\n"
        "ds_write_b128 v116, v[72:75] offset:1024\n"
        "v_pk_mul_f32 v[56:57], v[56:57], v[112:113] op_sel_hi:[1,0]\n"
        "v_pk_mul_f32 v[58:59], v[58:59], v[112:113] op_sel_hi:[1,0]\n"
        "v_cvt_pk_bf16_f32 v76, v56, v57\n"
        "v_cvt_pk_bf16_f32 v77, v58, v59\n"
        "ds_write_b128 v116, v[76:79] offset:1536\n"
        "s_cmp_lg_u32 s45, 2\n"
        "s_cbranch_scc1 L_compute_%=\n"
        "s_cmp_lg_u32 s67, 0\n"
        "s_cbranch_scc1 L_after_%=\n"
        "v_mov_b32 v5, v151\n"
        "s_waitcnt vmcnt(5) lgkmcnt(0)\n"
        "s_barrier\n"
        "s_cmp_eq_u32 s63, 7\n"
        "s_cbranch_scc1 L_nopub_%=\n"
        "s_lshl_b32 s46, s44, 12\n"
        "v_add_u32 v152, s46, v115\n"
        "ds_read_b128 v[10:13], v152 offset:16384\n"
        "ds_read_b128 v[14:17], v152 offset:17408\n"
        "ds_read_b128 v[18:21], v152 offset:18432\n"
        "ds_read_b128 v[22:25], v152 offset:19456\n"
        "s_lshl_b32 s46, s70, 14\n"
        "s_add_u32 s36, s56, s46\n"
        "s_addc_u32 s37, s57, 0\n"
        "s_waitcnt lgkmcnt(3)\n"
        "global_store_dwordx4 v115, v[10:13], s[36:37] offset:0 sc1\n"
        "s_waitcnt lgkmcnt(2)\n"
        "global_store_dwordx4 v115, v[14:17], s[36:37] offset:1024 sc1\n"
        "s_waitcnt lgkmcnt(1)\n"
        "global_store_dwordx4 v115, v[18:21], s[36:37] offset:2048 sc1\n"
        "s_waitcnt lgkmcnt(0)\n"
        "global_store_dwordx4 v115, v[22:25], s[36:37] offset:3072 sc1\n"
        "L_nopub_%=:\n"
        "s_mov_b32 s65, 0\n"
        "s_mov_b32 s67, 1\n"
        "s_mov_b32 s71, 2\n"
        "s_lshl_b32 s46, s63, 7\n"
        "v_lshl_add_u32 v152, v150, 3, s46\n"
        "s_waitcnt vmcnt(0)\n"
        "v_readfirstlane_b32 s60, v0\n"
        "v_readfirstlane_b32 s61, v1\n"
        "s_add_u32 s72, s72, 1\n"
        "s_cmp_eq_u32 s72, 8\n"
        "s_cselect_b32 s72, 1, s72\n"
        "s_add_u32 s60, s60, 0x7f4a7c15\n"
        "s_addc_u32 s61, s61, 0x9e3779b9\n"
        "v_mov_b32 v0, s60\n"
        "v_mov_b32 v1, s61\n"
        "s_mov_b64 exec, 0xffff\n"
        "global_store_dwordx2 v152, v[0:1], s[58:59] sc1\n"
        "s_mov_b64 exec, -1\n"
        "s_mov_b32 s53, s72\n"
        "s_mul_i32 s46, s72, 14336\n"
        "s_add_u32 s38, s68, s46\n"
        "s_addc_u32 s39, s69, 0\n"
        "s_add_u32 s52, s52, 14336\n"
        "s_sub_u32 s46, s52, 57344\n"
        "s_cmp_ge_u32 s52, 122880\n"
        "s_cselect_b32 s52, s46, s52\n"
        "s_mov_b32 m0, s52\n"
        "s_nop 0\n"
        "global_load_lds_dwordx4 v115, s[38:39] offset:0\n"
        "global_load_lds_dwordx4 v115, s[38:39] offset:1024\n"
        "global_load_lds_dwordx4 v115, s[38:39] offset:2048\n"
        "s_mov_b32 exec_hi, 0\n"
        "global_load_lds_dwordx4 v115, s[38:39] offset:3072\n"
        "s_mov_b32 exec_hi, -1\n"
        "s_barrier\n"
        "L_ploop_%=:\n"
        "s_cmp_eq_u32 s71, 7\n"
        "s_cbranch_scc1 L_nov_%=\n"
        "s_add_u32 s72, s72, 1\n"
        "s_cmp_eq_u32 s72, 8\n"
        "s_cselect_b32 s72, 1, s72\n"
        "s_mul_i32 s46, s72, 14336\n"
        "s_add_u32 s38, s68, s46\n"
        "s_addc_u32 s39, s69, 0\n"
        "s_add_u32 s52, s52, 14336\n"
        "s_sub_u32 s46, s52, 57344\n"
        "s_cmp_ge_u32 s52, 122880\n"
        "s_cselect_b32 s52, s46, s52\n"
        "s_mov_b32 m0, s52\n"
        "s_nop 0\n"
        "global_load_lds_dwordx4 v115, s[38:39] offset:0\n"
        "global_load_lds_dwordx4 v115, s[38:39] offset:1024\n"
        "global_load_lds_dwordx4 v115, s[38:39] offset:2048\n"
        "s_mov_b32 exec_hi, 0\n"
        "global_load_lds_dwordx4 v115, s[38:39] offset:3072\n"
        "s_mov_b32 exec_hi, -1\n"
        "L_nov_%=:\n"
        "s_sub_u32 s47, s53, 1\n"
        "s_bitcmp1_b32 s65, s47\n"
        "s_cbranch_scc1 L_have_%=\n"
        "s_mov_b32 s66, 3\n"
        "L_poll_%=:\n"
        "global_load_dwordx2 v[2:3], v4, s[58:59] sc1\n"
        "s_waitcnt vmcnt(0)\n"
        "v_cmp_eq_u64 vcc, s[60:61], v[2:3]\n"
        "s_nop 1\n"
        "s_and_b32 s65, vcc_lo, 255\n"
        "s_bitcmp1_b32 s65, s47\n"
        "s_cbranch_scc1 L_have_%=\n"
        "s_sub_u32 s66, s66, 1\n"
        "s_cmp_lg_u32 s66, 0\n"
        "s_cbranch_scc1 L_poll_%=\n"
        "s_mul_i32 s46, s53, 14336\n"
        "s_mov_b64 s[36:37], s[42:43]\n"
        "s_add_u32 s36, s36, s46\n"
        "s_addc_u32 s37, s37, 0\n"
        "global_load_dwordx4 v[96:99], v114, s[36:37]\n"
        "global_load_dwordx4 v[100:103], v114, s[36:37] offset:16\n"
        "global_load_dwordx4 v[104:107], v114, s[36:37] offset:32\n"
        "global_load_dwordx2 v[108:109], v114, s[36:37] offset:48\n"
        "s_waitcnt vmcnt(0)\n"
        "s_and_b32 s46, s71, 3\n"
        "s_lshl_b32 s46, s46, 2\n"
        "s_add_u32 s46, s46, s44\n"
        "s_lshl_b32 s46, s46, 12\n"
        "v_add_u32 v116, s46, v5\n"
        "s_branch L_norm_%=\n"
        "L_have_%=:\n"
        "s_and_b32 s46, s71, 3\n"
        "s_lshl_b32 s46, s46, 2\n"
        "s_add_u32 s46, s46, s44\n"
        "s_lshl_b32 m0, s46, 12\n"
        "s_lshl_b32 s46, s53, 14\n"
        "s_add_u32 s36, s56, s46\n"
        "s_addc_u32 s37, s57, 0\n"
        "global_load_lds_dwordx4 v115, s[36:37] offset:0 sc1\n"
        "global_load_lds_dwordx4 v115, s[36:37] offset:1024 sc1\n"
        "global_load_lds_dwordx4 v115, s[36:37] offset:2048 sc1\n"
        "global_load_lds_dwordx4 v115, s[36:37] offset:3072 sc1\n"
        "L_after_%=:\n"
        "s_waitcnt vmcnt(0) lgkmcnt(0)\n"
        "s_barrier\n"
        "s_add_u32 s53, s53, 1\n"
        "s_cmp_eq_u32 s53, 8\n"
        "s_cselect_b32 s53, 1, s53\n"
        "s_add_u32 s71, s71, 1\n"
        "s_cmp_lt_u32 s71, 8\n"
        "s_cbranch_scc1 L_ploop_%=\n"
        "s_cmp_eq_u32 s65, 0xff\n"
        "s_cbranch_scc1 L_epoch_%=\n"
        "global_load_dwordx2 v[2:3], v4, s[58:59] sc1\n"
        "s_waitcnt vmcnt(0)\n"
        "v_cmp_eq_u64 vcc, s[60:61], v[2:3]\n"
        "s_nop 1\n"
        "s_cmp_eq_u32 vcc_lo, -1\n"
        "s_cbranch_scc0 L_pend_%=\n"
        "L_epoch_%=:\n"
        "v_lshlrev_b32 v152, 3, v150\n"
        "s_mov_b64 exec, 0xffff\n"
        "global_store_dwordx2 v152, v[0:1], s[58:59] offset:1024 sc1\n"
        "L_pend_%=:\n"
        "s_endpgm\n"
        "L_compute_%=:\n"
        "s_lshl_b32 s46, s44, 12\n"
        "s_add_u32 s46, s46, 131072\n"
        "s_lshl_b32 s47, s45, 11\n"
        "v_add_u32 v152, s46, v115\n"
        "v_xor_b32 v153, s47, v152\n"
        "v_xor_b32 v152, 0x800, v153\n"
        "s_mov_b32 s49, 0\n"
        "s_mov_b32 s51, 8\n"
        "s_waitcnt lgkmcnt(0)\n"
        "s_barrier\n"
        "ds_read_b128 v[100:103], v152 offset:1024\n"
        "ds_read_b128 v[116:119], v145 offset:1024\n"
        "ds_read_b128 v[96:99], v152\n"
        "ds_read_b128 v[112:115], v145 offset:0\n"
        "ds_read_b128 v[108:111], v153 offset:1024\n"
        "ds_read_b128 v[104:107], v153\n"
        "s_waitcnt lgkmcnt(4)\n"
        "v_mfma_f32_32x32x16_bf16 v[32:47], v[116:119], v[100:103], 0\n"
        "s_waitcnt lgkmcnt(2)\n"
        "v_mfma_f32_32x32x16_bf16 v[32:47], v[112:115], v[96:99], v[32:47]\n"
        "ds_read_b128 v[120:123], v145 offset:2048\n"
        "ds_read_b128 v[124:127], v145 offset:3072\n"
        "s_nop 7\n"
        "s_waitcnt lgkmcnt(2)\n"
        "s_setprio 2\n"
        "s_branch L_h0_%=\n"
        ".p2align 6\n"
        "L_loop_%=:\n"
        "s_waitcnt lgkmcnt(4)\n"
        "v_mfma_f32_32x32x16_bf16 v[32:47], v[116:119], v[100:103], 0\n"
        "v_exp_f32 v80, v80\n"
        "v_exp_f32 v81, v81\n"
        "ds_read_b128 v[120:123], v145 offset:2048\n"
        "v_exp_f32 v82, v82\n"
        "v_exp_f32 v83, v83\n"
        "ds_read_b128 v[124:127], v145 offset:3072\n"
        "v_mfma_f32_32x32x16_bf16 v[32:47], v[112:115], v[96:99], v[32:47]\n"
        "v_exp_f32 v84, v84\n"
        "v_exp_f32 v85, v85\n"
        "v_exp_f32 v86, v86\n"
        "v_exp_f32 v87, v87\n"
        "s_waitcnt lgkmcnt(2)\n"
        "v_smfmac_f32_32x32x32_bf16 v[0:15], v[136:139], v[64:71], v144\n"
        "v_exp_f32 v88, v88\n"
        "v_exp_f32 v89, v89\n"
        "v_mad_u32_u24 v146, s49, v149, v148\n"
        "v_exp_f32 v90, v90\n"
        "v_exp_f32 v91, v91\n"
        "v_smfmac_f32_32x32x32_bf16 v[0:15], v[140:143], v[72:79], v144\n"
        "v_exp_f32 v92, v92\n"
        "v_exp_f32 v93, v93\n"
        "v_exp_f32 v94, v94\n"
        "v_exp_f32 v95, v95\n"
        "L_h0_%=:\n"
        "v_mfma_f32_32x32x16_bf16 v[48:63], v[116:119], v[108:111], 0\n"
        "v_exp_f32 v32, v32\n"
        "v_exp_f32 v33, v33\n"
        "ds_read_b64_tr_b16 v[128:129], v146 offset:0\n"
        "v_exp_f32 v34, v34\n"
        "v_exp_f32 v35, v35\n"
        "ds_read_b64_tr_b16 v[130:131], v146 offset:224\n"
        "v_mfma_f32_32x32x16_bf16 v[48:63], v[112:115], v[104:107], v[48:63]\n"
        "v_exp_f32 v36, v36\n"
        "v_exp_f32 v37, v37\n"
        "ds_read_b64_tr_b16 v[132:133], v146 offset:896\n"
        "v_exp_f32 v38, v38\n"
        "v_exp_f32 v39, v39\n"
        "s_setprio 2\n"
        "ds_read_b64_tr_b16 v[134:135], v146 offset:1120\n"
        "v_smfmac_f32_32x32x32_bf16 v[16:31], v[136:139], v[80:87], v144\n"
        "v_exp_f32 v40, v40\n"
        "v_exp_f32 v41, v41\n"
        "v_exp_f32 v42, v42\n"
        "v_exp_f32 v43, v43\n"
        "v_smfmac_f32_32x32x32_bf16 v[16:31], v[140:143], v[88:95], v144\n"
        "v_exp_f32 v44, v44\n"
        "v_exp_f32 v45, v45\n"
        "v_exp_f32 v46, v46\n"
        "v_exp_f32 v47, v47\n"
        "s_waitcnt lgkmcnt(4)\n"
        "v_mfma_f32_32x32x16_bf16 v[64:79], v[124:127], v[100:103], 0\n"
        "v_exp_f32 v48, v48\n"
        "v_exp_f32 v49, v49\n"
        "ds_read_b128 v[112:115], v145 offset:4096\n"
        "v_exp_f32 v50, v50\n"
        "v_exp_f32 v51, v51\n"
        "ds_read_b128 v[116:119], v145 offset:5120\n"
        "v_mfma_f32_32x32x16_bf16 v[64:79], v[120:123], v[96:99], v[64:79]\n"
        "v_exp_f32 v52, v52\n"
        "v_exp_f32 v53, v53\n"
        "v_exp_f32 v54, v54\n"
        "v_exp_f32 v55, v55\n"
        "s_waitcnt lgkmcnt(2)\n"
        "v_smfmac_f32_32x32x32_bf16 v[0:15], v[128:131], v[32:39], v144\n"
        "v_exp_f32 v56, v56\n"
        "v_exp_f32 v57, v57\n"
        "v_exp_f32 v58, v58\n"
        "v_exp_f32 v59, v59\n"
        "v_smfmac_f32_32x32x32_bf16 v[0:15], v[132:135], v[40:47], v144\n"
        "v_exp_f32 v60, v60\n"
        "v_exp_f32 v61, v61\n"
        "v_exp_f32 v62, v62\n"
        "v_exp_f32 v63, v63\n"
        "v_mfma_f32_32x32x16_bf16 v[80:95], v[124:127], v[108:111], 0\n"
        "v_exp_f32 v64, v64\n"
        "v_exp_f32 v65, v65\n"
        "ds_read_b64_tr_b16 v[136:137], v146 offset:1792\n"
        "v_exp_f32 v66, v66\n"
        "v_exp_f32 v67, v67\n"
        "ds_read_b64_tr_b16 v[138:139], v146 offset:2016\n"
        "v_mfma_f32_32x32x16_bf16 v[80:95], v[120:123], v[104:107], v[80:95]\n"
        "v_exp_f32 v68, v68\n"
        "v_exp_f32 v69, v69\n"
        "ds_read_b64_tr_b16 v[140:141], v146 offset:2688\n"
        "v_exp_f32 v70, v70\n"
        "v_exp_f32 v71, v71\n"
        "ds_read_b64_tr_b16 v[142:143], v146 offset:2912\n"
        "v_smfmac_f32_32x32x32_bf16 v[16:31], v[128:131], v[48:55], v144\n"
        "v_exp_f32 v72, v72\n"
        "v_exp_f32 v73, v73\n"
        "v_exp_f32 v74, v74\n"
        "v_exp_f32 v75, v75\n"
        "v_smfmac_f32_32x32x32_bf16 v[16:31], v[132:135], v[56:63], v144\n"
        "v_exp_f32 v76, v76\n"
        "v_exp_f32 v77, v77\n"
        "v_exp_f32 v78, v78\n"
        "v_exp_f32 v79, v79\n"
        "s_waitcnt lgkmcnt(4)\n"
        "v_mfma_f32_32x32x16_bf16 v[32:47], v[116:119], v[100:103], 0\n"
        "v_exp_f32 v80, v80\n"
        "v_exp_f32 v81, v81\n"
        "ds_read_b128 v[120:123], v145 offset:6144\n"
        "v_exp_f32 v82, v82\n"
        "v_exp_f32 v83, v83\n"
        "ds_read_b128 v[124:127], v145 offset:7168\n"
        "v_mfma_f32_32x32x16_bf16 v[32:47], v[112:115], v[96:99], v[32:47]\n"
        "v_exp_f32 v84, v84\n"
        "v_exp_f32 v85, v85\n"
        "v_exp_f32 v86, v86\n"
        "v_exp_f32 v87, v87\n"
        "s_setprio 1\n"
        "s_waitcnt lgkmcnt(2)\n"
        "v_smfmac_f32_32x32x32_bf16 v[0:15], v[136:139], v[64:71], v144\n"
        "v_exp_f32 v88, v88\n"
        "v_exp_f32 v89, v89\n"
        "v_exp_f32 v90, v90\n"
        "v_exp_f32 v91, v91\n"
        "v_smfmac_f32_32x32x32_bf16 v[0:15], v[140:143], v[72:79], v144\n"
        "v_exp_f32 v92, v92\n"
        "v_exp_f32 v93, v93\n"
        "v_exp_f32 v94, v94\n"
        "v_exp_f32 v95, v95\n"
        "v_mfma_f32_32x32x16_bf16 v[48:63], v[116:119], v[108:111], 0\n"
        "v_exp_f32 v32, v32\n"
        "v_exp_f32 v33, v33\n"
        "ds_read_b64_tr_b16 v[128:129], v146 offset:3584\n"
        "v_exp_f32 v34, v34\n"
        "v_exp_f32 v35, v35\n"
        "ds_read_b64_tr_b16 v[130:131], v146 offset:3808\n"
        "v_mfma_f32_32x32x16_bf16 v[48:63], v[112:115], v[104:107], v[48:63]\n"
        "v_exp_f32 v36, v36\n"
        "v_exp_f32 v37, v37\n"
        "ds_read_b64_tr_b16 v[132:133], v146 offset:4480\n"
        "v_exp_f32 v38, v38\n"
        "v_exp_f32 v39, v39\n"
        "s_setprio 0\n"
        "ds_read_b64_tr_b16 v[134:135], v146 offset:4704\n"
        "v_smfmac_f32_32x32x32_bf16 v[16:31], v[136:139], v[80:87], v144\n"
        "v_exp_f32 v40, v40\n"
        "v_exp_f32 v41, v41\n"
        "s_add_u32 s49, s49, 1\n"
        "s_and_b32 s49, s49, 3\n"
        "s_lshl_b32 s50, s49, 14\n"
        "v_exp_f32 v42, v42\n"
        "v_exp_f32 v43, v43\n"
        "v_add_u32 v145, s50, v147\n"
        "v_smfmac_f32_32x32x32_bf16 v[16:31], v[140:143], v[88:95], v144\n"
        "v_exp_f32 v44, v44\n"
        "v_exp_f32 v45, v45\n"
        "v_exp_f32 v46, v46\n"
        "v_exp_f32 v47, v47\n"
        "s_waitcnt lgkmcnt(4)\n"
        "v_mfma_f32_32x32x16_bf16 v[64:79], v[124:127], v[100:103], 0\n"
        "s_setprio 3\n"
        "s_barrier\n"
        "v_exp_f32 v48, v48\n"
        "v_exp_f32 v49, v49\n"
        "ds_read_b128 v[112:115], v145 offset:0\n"
        "v_exp_f32 v50, v50\n"
        "v_exp_f32 v51, v51\n"
        "ds_read_b128 v[116:119], v145 offset:1024\n"
        "v_mfma_f32_32x32x16_bf16 v[64:79], v[120:123], v[96:99], v[64:79]\n"
        "v_exp_f32 v52, v52\n"
        "v_exp_f32 v53, v53\n"
        "v_exp_f32 v54, v54\n"
        "v_exp_f32 v55, v55\n"
        "s_waitcnt lgkmcnt(2)\n"
        "v_smfmac_f32_32x32x32_bf16 v[0:15], v[128:131], v[32:39], v144\n"
        "v_exp_f32 v56, v56\n"
        "v_exp_f32 v57, v57\n"
        "v_exp_f32 v58, v58\n"
        "v_exp_f32 v59, v59\n"
        "v_smfmac_f32_32x32x32_bf16 v[0:15], v[132:135], v[40:47], v144\n"
        "v_exp_f32 v60, v60\n"
        "v_exp_f32 v61, v61\n"
        "v_exp_f32 v62, v62\n"
        "v_exp_f32 v63, v63\n"
        "v_mfma_f32_32x32x16_bf16 v[80:95], v[124:127], v[108:111], 0\n"
        "v_exp_f32 v64, v64\n"
        "v_exp_f32 v65, v65\n"
        "ds_read_b64_tr_b16 v[136:137], v146 offset:5376\n"
        "v_exp_f32 v66, v66\n"
        "v_exp_f32 v67, v67\n"
        "ds_read_b64_tr_b16 v[138:139], v146 offset:5600\n"
        "v_mfma_f32_32x32x16_bf16 v[80:95], v[120:123], v[104:107], v[80:95]\n"
        "v_exp_f32 v68, v68\n"
        "v_exp_f32 v69, v69\n"
        "ds_read_b64_tr_b16 v[140:141], v146 offset:6272\n"
        "v_exp_f32 v70, v70\n"
        "v_exp_f32 v71, v71\n"
        "ds_read_b64_tr_b16 v[142:143], v146 offset:6496\n"
        "v_smfmac_f32_32x32x32_bf16 v[16:31], v[128:131], v[48:55], v144\n"
        "v_exp_f32 v72, v72\n"
        "v_exp_f32 v73, v73\n"
        "v_exp_f32 v74, v74\n"
        "v_exp_f32 v75, v75\n"
        "s_add_i32 s51, s51, -1\n"
        "s_cmp_lg_u32 s51, 0\n"
        "v_smfmac_f32_32x32x32_bf16 v[16:31], v[132:135], v[56:63], v144\n"
        "v_exp_f32 v76, v76\n"
        "v_exp_f32 v77, v77\n"
        "v_exp_f32 v78, v78\n"
        "v_exp_f32 v79, v79\n"
        "s_cbranch_scc1 L_loop_%=\n"
        "s_setprio 0\n"
        "s_waitcnt lgkmcnt(0)\n"
        "v_smfmac_f32_32x32x32_bf16 v[0:15], v[136:139], v[64:71], v144\n"
        "v_exp_f32 v80, v80\n"
        "v_exp_f32 v81, v81\n"
        "v_smfmac_f32_32x32x32_bf16 v[0:15], v[140:143], v[72:79], v144\n"
        "v_exp_f32 v82, v82\n"
        "v_exp_f32 v83, v83\n"
        "v_exp_f32 v84, v84\n"
        "v_exp_f32 v85, v85\n"
        "v_exp_f32 v86, v86\n"
        "v_exp_f32 v87, v87\n"
        "v_exp_f32 v88, v88\n"
        "v_exp_f32 v89, v89\n"
        "v_exp_f32 v90, v90\n"
        "v_exp_f32 v91, v91\n"
        "v_exp_f32 v92, v92\n"
        "v_exp_f32 v93, v93\n"
        "ds_write_b128 v154, v[12:15] offset:3072\n"
        "v_exp_f32 v94, v94\n"
        "ds_write_b128 v154, v[0:3] offset:0\n"
        "v_exp_f32 v95, v95\n"
        "ds_write_b128 v154, v[4:7] offset:1024\n"
        "v_smfmac_f32_32x32x32_bf16 v[16:31], v[136:139], v[80:87], v144\n"
        "ds_write_b128 v154, v[8:11] offset:2048\n"
        "v_smfmac_f32_32x32x32_bf16 v[16:31], v[140:143], v[88:95], v144\n"
        "s_waitcnt lgkmcnt(0)\n"
        "s_barrier\n"
        "ds_read_b128 v[44:47], v155 offset:3072\n"
        "ds_read_b128 v[32:35], v155 offset:0\n"
        "ds_read_b128 v[36:39], v155 offset:1024\n"
        "ds_read_b128 v[40:43], v155 offset:2048\n"
        "s_nop 4\n"
        "s_waitcnt lgkmcnt(3)\n"
        "v_pk_add_f32 v[28:29], v[28:29], v[44:45]\n"
        "v_pk_add_f32 v[30:31], v[30:31], v[46:47]\n"
        "v_mov_b32 v48, v28\n"
        "v_mov_b32 v50, v28\n"
        "s_waitcnt lgkmcnt(2)\n"
        "v_pk_add_f32 v[16:17], v[16:17], v[32:33]\n"
        "v_permlane32_swap_b32 v48, v50\n"
        "v_pk_add_f32 v[18:19], v[18:19], v[34:35]\n"
        "s_waitcnt lgkmcnt(0)\n"
        "v_rcp_f32 v50, v50\n"
        "v_pk_add_f32 v[20:21], v[20:21], v[36:37]\n"
        "v_pk_add_f32 v[22:23], v[22:23], v[38:39]\n"
        "v_pk_add_f32 v[24:25], v[24:25], v[40:41]\n"
        "v_pk_add_f32 v[26:27], v[26:27], v[42:43]\n"
        "v_pk_mul_f32 v[16:17], v[16:17], v[50:51] op_sel_hi:[1,0]\n"
        "v_pk_mul_f32 v[18:19], v[18:19], v[50:51] op_sel_hi:[1,0]\n"
        "v_cvt_pk_bf16_f32 v64, v16, v17\n"
        "v_cvt_pk_bf16_f32 v65, v18, v19\n"
        "ds_write_b64 v156, v[64:65] offset:0\n"
        "v_pk_mul_f32 v[20:21], v[20:21], v[50:51] op_sel_hi:[1,0]\n"
        "v_pk_mul_f32 v[22:23], v[22:23], v[50:51] op_sel_hi:[1,0]\n"
        "v_cvt_pk_bf16_f32 v66, v20, v21\n"
        "v_cvt_pk_bf16_f32 v67, v22, v23\n"
        "ds_write_b64 v156, v[66:67] offset:16\n"
        "v_pk_mul_f32 v[24:25], v[24:25], v[50:51] op_sel_hi:[1,0]\n"
        "v_pk_mul_f32 v[26:27], v[26:27], v[50:51] op_sel_hi:[1,0]\n"
        "v_cvt_pk_bf16_f32 v68, v24, v25\n"
        "v_cvt_pk_bf16_f32 v69, v26, v27\n"
        "ds_write_b64 v156, v[68:69] offset:32\n"
        "v_pk_mul_f32 v[28:29], v[28:29], v[50:51] op_sel_hi:[1,0]\n"
        "v_pk_mul_f32 v[30:31], v[30:31], v[50:51] op_sel_hi:[1,0]\n"
        "v_cvt_pk_bf16_f32 v70, v28, v29\n"
        "v_cvt_pk_bf16_f32 v71, v30, v31\n"
        "s_mov_b32 exec_hi, 0\n"
        "ds_write_b64 v156, v[70:71] offset:48\n"
        "s_mov_b32 exec_hi, -1\n"
        "v_lshlrev_b32 v151, 4, v150\n"
        "s_waitcnt lgkmcnt(0)\n"
        "ds_read_b128 v[32:35], v157\n"
        "ds_read_b128 v[36:39], v157 offset:1024\n"
        "s_waitcnt lgkmcnt(1)\n"
        "global_store_dwordx4 v151, v[32:35], s[40:41] sc1 nt\n"
        "s_mov_b32 exec_hi, 0xffff\n"
        "s_waitcnt lgkmcnt(0)\n"
        "global_store_dwordx4 v151, v[36:39], s[40:41] offset:1024 sc1 nt\n"
        "s_endpgm\n"
        "L_fallback_%=:\n"
        "s_waitcnt vmcnt(0) lgkmcnt(0)\n"
        "s_setprio 0\n"
        :
        : [q] "s"(q), [k] "s"(k), [v] "s"(v), [t] "s"(t), [o] "s"(out), [bid] "s"(bid), [wid] "s"(wid), [ws] "s"(ws)
        : "v0", "v1", "v2", "v3", "v4", "v5", "v6", "v7", "v8", "v9", "v10", "v11", "v12", "v13", "v14", "v15", "v16", "v17", "v18", "v19", "v20", "v21", "v22", "v23", "v24", "v25", "v26", "v27", "v28", "v29", "v30", "v31", "v32", "v33", "v34", "v35", "v36", "v37", "v38", "v39", "v40", "v41", "v42", "v43", "v44", "v45", "v46", "v47", "v48", "v49", "v50", "v51", "v52", "v53", "v54", "v55", "v56", "v57", "v58", "v59", "v60", "v61", "v62", "v63", "v64", "v65", "v66", "v67", "v68", "v69", "v70", "v71", "v72", "v73", "v74", "v75", "v76", "v77", "v78", "v79", "v80", "v81", "v82", "v83", "v84", "v85", "v86", "v87", "v88", "v89", "v90", "v91", "v92", "v93", "v94", "v95", "v96", "v97", "v98", "v99", "v100", "v101", "v102", "v103", "v104", "v105", "v106", "v107", "v108", "v109", "v110", "v111", "v112", "v113", "v114", "v115", "v116", "v117", "v118", "v119", "v120", "v121", "v122", "v123", "v124", "v125", "v126", "v127", "v128", "v129", "v130", "v131", "v132", "v133", "v134", "v135", "v136", "v137", "v138", "v139", "v140", "v141", "v142", "v143", "v144", "v145", "v146", "v147", "v148", "v149", "v150", "v151", "v152", "v153", "v154", "v155", "v156", "v157", "s36", "s37", "s38", "s39", "s40", "s41", "s42", "s43", "s44", "s45", "s46", "s47", "s48", "s49", "s50", "s51", "s52", "s53", "s54", "s55", "s56", "s57", "s58", "s59", "s60", "s61", "s62", "s63", "s64", "s65", "s66", "s67", "s68", "s69", "s70", "s71", "s72", "s73", "vcc", "scc", "memory");
    const int bh = ((bid & 7) + 8 * (bid >> 6)) ^ ((bid >> 5) & 4), qb = (bid >> 3) & 7;
    const int tid = wid * 64 + __builtin_amdgcn_mbcnt_hi(~0u, __builtin_amdgcn_mbcnt_lo(~0u, 0u));
    slow_path(q, k, v, bf2f(t[bh % NH]), out, bh, qb, tid, (float*)smem);
}

extern "C" void kernel_launch(void* const* d_in, const int* in_sizes, int n_in, void* d_out, int out_size, void* d_ws,
                              size_t ws_size, hipStream_t stream) {
    qk_norm_attn<<<256, 768, 0, stream>>>((const unsigned short*)d_in[0], (const unsigned short*)d_in[1],
                                          (const unsigned short*)d_in[2], (const unsigned short*)d_in[3], (unsigned short*)d_out, d_ws);
}
```
